```python
import torch

def _softmax(x, axis=-1):
    return torch.softmax(x, dim=axis)
def _gelu(x):
    return torch.nn.functional.gelu(x, approximate="tanh")
def _silu(x):
    return torch.nn.functional.silu(x)
def _sigmoid(x):
    return torch.sigmoid(x)
def _layernorm(x, axis=-1, eps=1e-5):
    m = x.mean(dim=axis, keepdim=True)
    v = x.var(dim=axis, keepdim=True, unbiased=False)
    return (x - m) / torch.sqrt(v + eps)
def _scatter_add(tgt, idx, src, axis=0):
    return tgt.clone().index_add_(axis, idx, src)
def _segment_sum(src, seg, n):
    out = torch.zeros((n,) + src.shape[1:], dtype=src.dtype)
    return out.index_add_(0, seg, src)
def _pad(x, pw):
    flat = tuple(v for pair in reversed(pw) for v in pair)
    return torch.nn.functional.pad(x, flat)

kernel_name = 'gather_scatter__edge_featured_mpnn_gatedgcn__graph'

def setup_inputs():

    _w = (lambda a: (lambda t: t.to(torch.bfloat16) if t.is_floating_point() else t)(torch.as_tensor(a)))
    N, deg, D, De = 50000, 16, 64, max(16, 1)
    E = N * deg
    return {
        "x": _w((lambda *s: torch.randn(*s, dtype=torch.bfloat16) * 0.4)(N, D)),
        "e_attr": _w((lambda *s: torch.randn(*s, dtype=torch.bfloat16) * 0.8)(E, De)),
        "src": _w((lambda lo, hi, *s: torch.randint(lo, hi, s))(0, N, E).to(torch.int64)),
        "dst": _w((lambda lo, hi, *s: torch.randint(lo, hi, s))(0, N, E).to(torch.int64)),
        "w_a": _w((lambda *s: torch.randn(*s, dtype=torch.bfloat16) * 0.8)(D, D)),
        "w_b": _w((lambda *s: torch.randn(*s, dtype=torch.bfloat16) * 0.4)(D, D)),
        "w_c": _w((lambda *s: torch.randn(*s, dtype=torch.bfloat16) * 0.8)(De, D)),
        "w_u": _w((lambda *s: torch.randn(*s, dtype=torch.bfloat16) * 0.05)(D, D)),
        "w_v": _w((lambda *s: torch.randn(*s, dtype=torch.bfloat16) * 0.05)(D, D)),
    }

def reference(x, e_attr, src, dst, w_a, w_b, w_c, w_u, w_v):
    N, D = x.shape
    hv = x @ w_v
    g = x[dst] @ w_a + x[src] @ w_b + e_attr @ w_c
    eta = _sigmoid(g)
    denom = _segment_sum(eta, dst, N) + 1e-6
    eta_n = eta / denom[dst]
    agg = _segment_sum(eta_n * hv[src], dst, N)
    z = x @ w_u + agg
    relu_z = torch.where(z > 0, z, 0 * z)
    return x + relu_z
```

```cpp
#include <hip/hip_runtime.h>
#include <hip/hip_bf16.h>
#include <stdint.h>

typedef __attribute__((ext_vector_type(8))) short bf16x8;
typedef __attribute__((ext_vector_type(4))) short s16x4;
typedef __attribute__((ext_vector_type(16))) float f32x16;
typedef __attribute__((ext_vector_type(2))) float f32x2;
typedef __attribute__((ext_vector_type(4))) unsigned u32x4;
typedef __attribute__((ext_vector_type(2))) unsigned u32x2;
typedef __attribute__((ext_vector_type(2))) __bf16 bf16x2_t;

#define DI __device__ __forceinline__
template <int V> struct IC { static constexpr int value = V; };

constexpr int NB = 256;
constexpr int NG = 8;
constexpr int SUB = 4;
constexpr int CS = 784;
constexpr int K1T = 1024;
constexpr int K2T = 512;
constexpr int CAP = 3840;

constexpr int L_IMG = 0;
constexpr int L_XB = L_IMG + NG * 2048;
constexpr int L_EB = L_XB + NG * 2 * 4096;
constexpr int L_SRC = L_EB + NG * 2 * 1024;
constexpr int L_EOF = L_SRC + CAP * 4;
constexpr int L_OHA = L_EOF + CAP * 4;
constexpr int L_CNT = L_OHA + CAP * 2;
constexpr int L_GB = L_CNT + 256;
constexpr int L_END = L_GB + 80;

DI unsigned pack_bf16(float a, float b) {
  f32x2 v = {a, b};
  bf16x2_t r = __builtin_convertvector(v, bf16x2_t);
  return __builtin_bit_cast(unsigned, r);
}
DI f32x16 mfma32(bf16x8 a, bf16x8 b, f32x16 c) { return __builtin_amdgcn_mfma_f32_32x32x16_bf16(a, b, c, 0, 0, 0); }
DI bf16x8 as_frag(u32x4 v) { return __builtin_bit_cast(bf16x8, v); }

DI void glds16(unsigned voff, const void* gbase, unsigned ldsaddr) {
  asm volatile("s_mov_b32 m0, %2\n\ts_nop 0\n\tglobal_load_lds_dwordx4 %0, %1" ::"v"(voff), "s"(gbase), "s"(ldsaddr)
               : "memory");
}

__global__ __launch_bounds__(K1T) void k1_partition(const int* __restrict__ src, const int* __restrict__ dst,
                                                    const u32x4* __restrict__ eattr, const uint16_t* __restrict__ wa,
                                                    const uint16_t* __restrict__ wb, const uint16_t* __restrict__ wc,
                                                    const uint16_t* __restrict__ wu, const uint16_t* __restrict__ wv,
                                                    unsigned* __restrict__ recs, u32x4* __restrict__ eperm,
                                                    uint16_t* __restrict__ tab, u32x4* __restrict__ wfrag, int E,
                                                    int BS, unsigned magicBS, int GS, unsigned magicGS, int P1) {
  __shared__ __attribute__((aligned(16))) char smem[SUB * CS * 32 + SUB * CS * 4 + SUB * 260 * 4];
  u32x4* stage = (u32x4*)smem;
  unsigned* recs_s = (unsigned*)(smem + SUB * CS * 32);
  unsigned* hist = (unsigned*)(smem + SUB * CS * 36);
  const int tid = threadIdx.x;
  if ((int)blockIdx.x == P1) {
    for (int it = tid; it < 34 * 64; it += K1T) {
      const int f = it >> 6, l = it & 63, c = l & 31, h = l >> 5;
      const uint16_t* w;
      int s, n;
      if (f < 32) {
        const int m = f >> 3;
        s = (f >> 1) & 3;
        n = f & 1;
        w = m == 0 ? wa : m == 1 ? wb : m == 2 ? wv : wu;
      } else {
        s = 0;
        n = f & 1;
        w = wc;
      }
      u32x4 v;
#pragma unroll
      for (int j = 0; j < 4; ++j) {
        const unsigned lo = w[(16 * s + 8 * h + 2 * j) * 64 + 2 * c + n];
        const unsigned hi = w[(16 * s + 8 * h + 2 * j + 1) * 64 + 2 * c + n];
        v[j] = lo | (hi << 16);
      }
      wfrag[it] = v;
    }
    return;
  }
  const int PT = P1 * SUB;
  const int base = blockIdx.x * (SUB * CS);
  int d[SUB], s[SUB];
  u32x4 ea[SUB][2];
  bool valid[SUB];
#pragma unroll
  for (int k = 0; k < SUB; ++k) {
    const int e = base + k * CS + tid;
    valid[k] = tid < CS && e < E;
    d[k] = valid[k] ? dst[e] : 0;
    s[k] = valid[k] ? src[e] : 0;
  }
#pragma unroll
  for (int k = 0; k < SUB; ++k) {
    const int e = base + k * CS + tid;
    if (valid[k]) {
      ea[k][0] = eattr[2 * (size_t)e];
      ea[k][1] = eattr[2 * (size_t)e + 1];
    }
  }
  for (int i = tid; i < SUB * 260; i += K1T) hist[i] = 0;
  __syncthreads();
  unsigned bk[SUB], rank[SUB], rec[SUB];
#pragma unroll
  for (int k = 0; k < SUB; ++k) {
    const unsigned dd = (unsigned)d[k];
    const unsigned b = __umulhi(dd, magicBS);
    const unsigned loc = dd - b * BS;
    const unsigned g = __umulhi(loc, magicGS);
    const unsigned sl = loc - g * GS;
    bk[k] = b;
    rec[k] = (unsigned)s[k] | (sl << 24) | (g << 29);
    if (valid[k]) rank[k] = atomicAdd(&hist[k * 260 + b], 1u);
  }
  __syncthreads();
  if (tid < SUB * 64) {
    const int k = tid >> 6, l = tid & 63;
    unsigned* hp = hist + k * 260 + 4 * l;
    const unsigned c0 = hp[0], c1 = hp[1], c2 = hp[2], c3 = hp[3];
    const unsigned tot = c0 + c1 + c2 + c3;
    unsigned inc = tot;
#pragma unroll
    for (int o = 1; o < 64; o <<= 1) {
      const unsigned t = __shfl_up(inc, o, 64);
      if (l >= o) inc += t;
    }
    const unsigned ex = inc - tot;
    hp[0] = ex;
    hp[1] = ex + c0;
    hp[2] = ex + c0 + c1;
    hp[3] = ex + c0 + c1 + c2;
    if (l == 63) hist[k * 260 + 256] = inc;
  }
  __syncthreads();
  if (tid <= NB) {
    u32x2 t;
    t[0] = hist[tid] | (hist[260 + tid] << 16);
    t[1] = hist[520 + tid] | (hist[780 + tid] << 16);
    *(u32x2*)(tab + (size_t)tid * PT + blockIdx.x * SUB) = t;
  }
#pragma unroll
  for (int k = 0; k < SUB; ++k) {
    if (valid[k]) {
      const unsigned pos = hist[k * 260 + bk[k]] + rank[k];
      recs_s[k * CS + pos] = rec[k];
      stage[k * CS * 2 + 2 * pos] = ea[k][0];
      stage[k * CS * 2 + 2 * pos + 1] = ea[k][1];
    }
    __syncthreads();
    const int cnt = (int)hist[k * 260 + 256];
    const size_t ob = (size_t)(base + k * CS);
    for (int i = tid; i < 2 * cnt; i += K1T) eperm[2 * ob + i] = stage[k * CS * 2 + i];
    const int r4 = tid - (K1T - CS / 4);
    if (r4 >= 0 && 4 * r4 < cnt) *(u32x4*)(recs + ob + 4 * r4) = *(const u32x4*)(recs_s + k * CS + 4 * r4);
  }
}

__global__ __launch_bounds__(K2T, 2) void k2_main(const uint16_t* __restrict__ x, const unsigned* __restrict__ recs,
                                                  const char* __restrict__ eperm, const uint16_t* __restrict__ tab,
                                                  const u32x4* __restrict__ wfrag, uint16_t* __restrict__ out, int N,
                                                  int BS, int GS, int PT) {
  __shared__ __attribute__((aligned(16))) char smem[L_END];
  const int tid = threadIdx.x, lane = tid & 63;
  const int wave = __builtin_amdgcn_readfirstlane(tid >> 6);
  const int c = lane & 31, h = lane >> 5;
  const int b = (blockIdx.x & 7) * (NB / 8) + (blockIdx.x >> 3);
  const unsigned sbase = (unsigned)(size_t)smem;
  unsigned* cnt = (unsigned*)(smem + L_CNT);
  unsigned* gb = (unsigned*)(smem + L_GB);
  unsigned* l_src = (unsigned*)(smem + L_SRC);
  unsigned* l_eof = (unsigned*)(smem + L_EOF);
  uint16_t* l_oha = (uint16_t*)(smem + L_OHA);

  const int node0 = b * BS + wave * GS;
  int nn = min(GS, BS - wave * GS);
  nn = min(nn, N - node0);

  {
    u32x4 z = {0, 0, 0, 0};
    *(u32x4*)(smem + L_IMG + tid * 32) = z;
    *(u32x4*)(smem + L_IMG + tid * 32 + 16) = z;
  }

  bf16x8 WB[4][2], WV[4][2], WC[2], AB[2][2];
#pragma unroll
  for (int s = 0; s < 4; ++s)
#pragma unroll
    for (int n = 0; n < 2; ++n) {
      WB[s][n] = as_frag(wfrag[((1 * 4 + s) * 2 + n) * 64 + lane]);
      WV[s][n] = as_frag(wfrag[((2 * 4 + s) * 2 + n) * 64 + lane]);
    }
  WC[0] = as_frag(wfrag[32 * 64 + lane]);
  WC[1] = as_frag(wfrag[33 * 64 + lane]);
  {
    const int sl = (c & 0x13) | ((c & 4) << 1) | ((c & 8) >> 1);
    int nd = node0 + sl;
    nd = min(max(nd, 0), N - 1);
    f32x16 acc[2];
#pragma unroll
    for (int n = 0; n < 2; ++n)
#pragma unroll
      for (int i = 0; i < 16; ++i) acc[n][i] = 0.f;
#pragma unroll
    for (int s = 0; s < 4; ++s) {
      const bf16x8 xa = as_frag(*(const u32x4*)(x + (size_t)nd * 64 + 16 * s + 8 * h));
#pragma unroll
      for (int n = 0; n < 2; ++n) acc[n] = mfma32(xa, as_frag(wfrag[((0 * 4 + s) * 2 + n) * 64 + lane]), acc[n]);
    }
#pragma unroll
    for (int n = 0; n < 2; ++n)
#pragma unroll
      for (int s = 0; s < 2; ++s) {
        u32x4 p;
#pragma unroll
        for (int j = 0; j < 4; ++j) p[j] = pack_bf16(acc[n][8 * s + 2 * j], acc[n][8 * s + 2 * j + 1]);
        AB[s][n] = as_frag(p);
      }
  }

  f32x16 den[2], num[2];
#pragma unroll
  for (int n = 0; n < 2; ++n)
#pragma unroll
    for (int i = 0; i < 16; ++i) den[n][i] = 0.f, num[n][i] = 0.f;

  const unsigned img = sbase + L_IMG + wave * 2048;
  const uint16_t* tab0 = tab + (size_t)b * PT;
  const uint16_t* tab1 = tab0 + PT;

  if (tid < 64) cnt[tid] = 0;
  if (tid == 0) gb[16] = 0;
  __syncthreads();
  {
    unsigned t = 0;
    for (int p = tid; p < PT; p += K2T) t += (unsigned)tab1[p] - (unsigned)tab0[p];
#pragma unroll
    for (int o = 32; o > 0; o >>= 1) t += __shfl_xor(t, o, 64);
    if (lane == 0) atomicAdd(&gb[16], t);
  }
  __syncthreads();
  const int total = (int)gb[16];
  const int step = (total + NG * 31 <= CAP) ? PT : 4;

  for (int p0 = 0; p0 < PT; p0 += step) {
    const int p1 = min(PT, p0 + step);
    int tid = threadIdx.x;
    asm volatile("" : "+v"(tid));
    const int lane = tid & 63;
    const int per = (p1 - p0 + K2T - 1) / K2T;
    const int pb = p0 + tid * per, pe = min(p1, pb + per);
    for (int p = pb; p < pe; ++p) {
      const int st = tab0[p], en = tab1[p];
      const unsigned* r = recs + (size_t)p * CS;
      for (int i = st; i < en; ++i) atomicAdd(&cnt[(r[i] >> 29) * 8 + wave], 1u);
    }
    __syncthreads();
    if (tid < 64) {
      const unsigned v = cnt[tid];
      unsigned inc = v;
#pragma unroll
      for (int o = 1; o < 8; o <<= 1) {
        const unsigned t = __shfl_up(inc, o, 8);
        if ((tid & 7) >= o) inc += t;
      }
      const unsigned gt = __shfl(inc, 7, 8);
      const unsigned gp = (gt + 31u) & ~31u;
      unsigned ginc = gp;
#pragma unroll
      for (int o = 8; o < 64; o <<= 1) {
        const unsigned t = __shfl_up(ginc, o, 64);
        if (tid >= o) ginc += t;
      }
      const unsigned gbase = ginc - gp;
      cnt[tid] = gbase + inc - v;
      if ((tid & 7) == 0) {
        gb[tid >> 3] = gbase;
        gb[8 + (tid >> 3)] = gt;
      }
    }
    __syncthreads();
    for (int p = pb; p < pe; ++p) {
      const int st = tab0[p], en = tab1[p];
      const unsigned* r = recs + (size_t)p * CS;
      for (int i = st; i < en; ++i) {
        const unsigned rc = r[i];
        const unsigned g = rc >> 29, sl = (rc >> 24) & 31u;
        const unsigned pos = atomicAdd(&cnt[g * 8 + wave], 1u);
        const unsigned e = pos & 31u;
        l_src[(pos & ~31u) + (e & 7u) * 4 + (e >> 3)] = (rc & 0xFFFFFFu) * 128u;
        l_eof[pos] = ((unsigned)p * CS + (unsigned)i) * 32u + ((e >> 3) & 1u) * 16u;
        l_oha[pos] = (uint16_t)(sbase + L_IMG + g * 2048 + e * 64 + ((sl * 2) ^ (((e >> 2) & 3u) << 4)));
      }
    }
    const unsigned gbase = gb[wave], gt = gb[8 + wave];
    const int nt = (int)((gt + 31u) >> 5);
    {
      const unsigned pos = gbase + gt + lane;
      if (pos < gbase + (unsigned)nt * 32u) {
        const unsigned e = pos & 31u;
        l_src[(pos & ~31u) + (e & 7u) * 4 + (e >> 3)] = 0;
        l_eof[pos] = ((e >> 3) & 1u) * 16u;
        l_oha[pos] = (uint16_t)(img + e * 64 + (62u ^ (((e >> 2) & 3u) << 4)));
      }
    }
    __syncthreads();
    if (tid < 64) cnt[tid] = 0;

    {
    int lane = threadIdx.x & 63;
    asm volatile("" : "+v"(lane));
    const int c = lane & 31, h = lane >> 5;
    const unsigned xb = sbase + L_XB + wave * 8192;
    const unsigned eb = sbase + L_EB + wave * 2048;
    unsigned xsw[4];
#pragma unroll
    for (int i = 0; i < 4; ++i) xsw[i] = (unsigned)(((lane & 7) ^ ((4 * i + (lane >> 4)) & 7)) * 16);
    const unsigned esw = (lane & 1) * 16;
    unsigned xrd[4];
#pragma unroll
    for (int s = 0; s < 4; ++s) xrd[s] = (unsigned)(L_XB + wave * 8192 + c * 128 + (((2 * s + h) ^ ((c >> 1) & 7)) * 16));
    const unsigned erd = (unsigned)(L_EB + wave * 2048 + c * 32 + ((h ^ ((c >> 3) & 1)) * 16));
    const unsigned ohr0 = (unsigned)(L_IMG + wave * 2048 + c * 64 + (((0 + h) ^ ((c >> 2) & 3)) * 16));
    const unsigned ohr1 = (unsigned)(L_IMG + wave * 2048 + c * 64 + (((2 + h) ^ ((c >> 2) & 3)) * 16));
    unsigned trd[2];
    {
      const int q = (lane >> 2) & 3, p = lane & 3, cb = (lane >> 4) & 1;
#pragma unroll
      for (int jh = 0; jh < 2; ++jh)
        trd[jh] = img + (unsigned)((8 * jh + 4 * h + q) * 64 + (((2 * cb + (p >> 1)) ^ ((2 * jh + h) & 3)) * 16) + 8 * (p & 1));
    }


    auto issue = [&](int t, int buf) {
      const unsigned lb = gbase + (unsigned)t * 32u;
      const u32x4 so = *(const u32x4*)(l_src + lb + (lane >> 3) * 4);
      const unsigned eo = l_eof[lb + (lane >> 1)];
      const unsigned xd = xb + (unsigned)buf * 4096u;
#pragma unroll
      for (int i = 0; i < 4; ++i) glds16(so[i] + xsw[i], x, xd + 1024u * i);
      glds16(eo ^ esw, eperm, eb + (unsigned)buf * 1024u);
    };
    auto tile = [&](int t, auto BUFC) {
      constexpr int BUF = decltype(BUFC)::value;
      const unsigned oha = l_oha[gbase + t * 32 + c];
      *(uint16_t*)(smem + (oha - sbase)) = 0x3F80;
      if (t + 1 < nt) asm volatile("s_waitcnt vmcnt(5)" ::: "memory");
      else asm volatile("s_waitcnt vmcnt(0)" ::: "memory");
      bf16x8 xf[4];
#pragma unroll
      for (int s = 0; s < 4; ++s) xf[s] = as_frag(*(const u32x4*)(smem + xrd[s] + BUF * 4096));
      const bf16x8 ef = as_frag(*(const u32x4*)(smem + erd + BUF * 1024));
      const bf16x8 oh0 = as_frag(*(const u32x4*)(smem + ohr0));
      const bf16x8 oh1 = as_frag(*(const u32x4*)(smem + ohr1));
      s16x4 t00, t01, t10, t11;
      asm volatile(
          "ds_read_b64_tr_b16 %0, %4\n\tds_read_b64_tr_b16 %1, %5\n\t"
          "ds_read_b64_tr_b16 %2, %4 offset:1024\n\tds_read_b64_tr_b16 %3, %5 offset:1024\n\t"
          "s_waitcnt lgkmcnt(0)"
          : "=&v"(t00), "=&v"(t01), "=&v"(t10), "=&v"(t11)
          : "v"(trd[0]), "v"(trd[1])
          : "memory");
      *(uint16_t*)(smem + (oha - sbase)) = 0;
      bf16x8 st[2];
      st[0] = __builtin_shufflevector(t00, t01, 0, 1, 2, 3, 4, 5, 6, 7);
      st[1] = __builtin_shufflevector(t10, t11, 0, 1, 2, 3, 4, 5, 6, 7);
      if (t + 2 < nt) issue(t + 2, BUF);

#pragma unroll
      for (int n = 0; n < 2; ++n) {
        f32x16 g, hv;
#pragma unroll
        for (int i = 0; i < 16; ++i) g[i] = 0.f, hv[i] = 0.f;
        g = mfma32(oh0, AB[0][n], g);
        g = mfma32(oh1, AB[1][n], g);
        g = mfma32(ef, WC[n], g);
#pragma unroll
        for (int s = 0; s < 4; ++s) g = mfma32(xf[s], WB[s][n], g);
#pragma unroll
        for (int s = 0; s < 4; ++s) hv = mfma32(xf[s], WV[s][n], hv);
#pragma unroll
        for (int s = 0; s < 2; ++s) {
          f32x2 a[4];
#pragma unroll
          for (int i = 0; i < 4; ++i) {
            f32x2 gg = {g[8 * s + 2 * i], g[8 * s + 2 * i + 1]};
            a[i] = gg * -1.4426950408889634f;
          }
#pragma unroll
          for (int i = 0; i < 4; ++i) a[i] = f32x2{__builtin_amdgcn_exp2f(a[i][0]), __builtin_amdgcn_exp2f(a[i][1])};
#pragma unroll
          for (int i = 0; i < 4; ++i) a[i] = a[i] + 1.0f;
#pragma unroll
          for (int i = 0; i < 4; ++i) a[i] = f32x2{__builtin_amdgcn_rcpf(a[i][0]), __builtin_amdgcn_rcpf(a[i][1])};
          u32x4 pe2, pm2;
#pragma unroll
          for (int i = 0; i < 4; ++i) {
            f32x2 hh = {hv[8 * s + 2 * i], hv[8 * s + 2 * i + 1]};
            f32x2 m = a[i] * hh;
            pe2[i] = pack_bf16(a[i][0], a[i][1]);
            pm2[i] = pack_bf16(m[0], m[1]);
          }
          den[n] = mfma32(st[s], as_frag(pe2), den[n]);
          num[n] = mfma32(st[s], as_frag(pm2), num[n]);
        }
      }
    };
    if (nt > 0) issue(0, 0);
    if (nt > 1) issue(1, 1);
    for (int t = 0; t < nt; t += 2) {
      tile(t, IC<0>{});
      if (t + 1 < nt) tile(t + 1, IC<1>{});
    }
    }
    __syncthreads();
  }

  if (nn <= 0) return;
  {
    int lane = threadIdx.x & 63;
    asm volatile("" : "+v"(lane));
    const int c = lane & 31, h = lane >> 5;
    int nd = node0 + c;
    nd = min(nd, N - 1);
    f32x16 u[2];
#pragma unroll
    for (int n = 0; n < 2; ++n)
#pragma unroll
      for (int i = 0; i < 16; ++i) u[n][i] = 0.f;
#pragma unroll
    for (int s = 0; s < 4; ++s) {
      const bf16x8 xa = as_frag(*(const u32x4*)(x + (size_t)nd * 64 + 16 * s + 8 * h));
#pragma unroll
      for (int n = 0; n < 2; ++n) u[n] = mfma32(xa, as_frag(wfrag[((3 * 4 + s) * 2 + n) * 64 + lane]), u[n]);
    }
#pragma unroll
    for (int i = 0; i < 16; ++i) {
      const int row = (i & 3) + 8 * (i >> 2) + 4 * h;
      if (row < nn) {
        const size_t o = (size_t)(node0 + row) * 64 + 2 * c;
        const unsigned xv = *(const unsigned*)(x + o);
        const float x0 = __uint_as_float(xv << 16), x1 = __uint_as_float(xv & 0xFFFF0000u);
        const float z0 = u[0][i] + num[0][i] / (den[0][i] + 1e-6f);
        const float z1 = u[1][i] + num[1][i] / (den[1][i] + 1e-6f);
        const float r0 = z0 > 0.f ? z0 : 0.f * z0;
        const float r1 = z1 > 0.f ? z1 : 0.f * z1;
        *(unsigned*)(out + o) = pack_bf16(x0 + r0, x1 + r1);
      }
    }
  }
}

extern "C" void kernel_launch(void* const* d_in, const int* in_sizes, int n_in, void* d_out, int out_size, void* d_ws,
                              size_t ws_size, hipStream_t stream) {
  const uint16_t* x = (const uint16_t*)d_in[0];
  const u32x4* eattr = (const u32x4*)d_in[1];
  const int* src = (const int*)d_in[2];
  const int* dst = (const int*)d_in[3];
  const uint16_t* wa = (const uint16_t*)d_in[4];
  const uint16_t* wb = (const uint16_t*)d_in[5];
  const uint16_t* wc = (const uint16_t*)d_in[6];
  const uint16_t* wu = (const uint16_t*)d_in[7];
  const uint16_t* wv = (const uint16_t*)d_in[8];
  const int N = in_sizes[0] / 64;
  const int E = in_sizes[2];
  const int BS = (N + NB - 1) / NB;
  const int GS = (BS + NG - 1) / NG;
  const unsigned magicBS = (unsigned)(0x100000000ull / (unsigned)BS) + 1u;
  const unsigned magicGS = (unsigned)(0x100000000ull / (unsigned)GS) + 1u;
  const int P1 = (E + SUB * CS - 1) / (SUB * CS);
  const int PT = P1 * SUB;
  char* ws = (char*)d_ws;
  size_t off = 0;
  u32x4* eperm = (u32x4*)(ws + off);
  off += (size_t)PT * CS * 32;
  unsigned* recs = (unsigned*)(ws + off);
  off += (size_t)PT * CS * 4;
  off = (off + 255) & ~(size_t)255;
  uint16_t* tab = (uint16_t*)(ws + off);
  off += (size_t)(NB + 1) * PT * 2;
  off = (off + 255) & ~(size_t)255;
  u32x4* wfrag = (u32x4*)(ws + off);
  k1_partition<<<P1 + 1, K1T, 0, stream>>>(src, dst, eattr, wa, wb, wc, wu, wv, recs, eperm, tab, wfrag, E, BS, magicBS,
                                           GS, magicGS, P1);
  k2_main<<<NB, K2T, 0, stream>>>(x, recs, (const char*)eperm, tab, wfrag, (uint16_t*)d_out, N, BS, GS, PT);
}
```

```cpp
#include <hip/hip_runtime.h>
#include <hip/hip_bf16.h>
#include <stdint.h>

typedef __attribute__((ext_vector_type(8))) short bf16x8;
typedef __attribute__((ext_vector_type(4))) short s16x4;
typedef __attribute__((ext_vector_type(16))) float f32x16;
typedef __attribute__((ext_vector_type(2))) float f32x2;
typedef __attribute__((ext_vector_type(4))) unsigned u32x4;
typedef __attribute__((ext_vector_type(2))) unsigned u32x2;
typedef __attribute__((ext_vector_type(2))) __bf16 bf16x2_t;

#define DI __device__ __forceinline__
template <int V> struct IC { static constexpr int value = V; };
#define SB() __builtin_amdgcn_sched_barrier(0)

constexpr float NLOG2E = -1.4426950408889634f;
constexpr int NB = 256;
constexpr int NG = 8;
constexpr int SUB = 1;
constexpr int NPH = 1;
constexpr int CSM = 3136 / SUB;
constexpr int NBIN = NPH * NB * NG;
constexpr int NQ = NB * SUB;
constexpr int NSEG = NPH * NQ;
#define TABIDX(b, sg) ((sg) * (NB + 8) + (b))
#define GSTART(gg, N) (((unsigned)(gg) * (unsigned)(N) + (unsigned)(NB * NG - 1)) / (unsigned)(NB * NG))
constexpr int K1T = 1024;
constexpr int K2T = 512;
constexpr int SPT = NSEG >= K2T ? NSEG / K2T : 1;
constexpr int TPS = NSEG >= K2T ? 1 : K2T / NSEG;
constexpr int RPL = 8;
constexpr int CAP = 3744;
constexpr int LIM = CAP - NG * 31;

constexpr size_t WS_RECS = 0;
constexpr size_t WS_TAB = WS_RECS + (size_t)NQ * CSM * 8 + 256;
constexpr size_t WS_WFRAG = WS_TAB + (size_t)(NB + 64) * (NSEG + 64) * 16;

constexpr int L_IMG = 0;
constexpr int L_EB = L_IMG + NG * 2048;
constexpr int L_LIST = L_EB + NG * 2 * 1024;
constexpr int L_XB = L_LIST + CAP * 8;
constexpr int L_WA = L_XB + NG * 2 * 4096;
constexpr int L_WV = L_WA + 16384;
constexpr int L_WU = L_WV + 8192;
constexpr int L_WC = L_WU + 8192;
constexpr int L_SCAN = L_WC + 2048;
static_assert(L_LIST == 32768 && CSM * 8 <= L_LIST, "list placement");
constexpr int L_MISC = L_SCAN + 256;
constexpr int L_PROG = L_MISC + 16;
constexpr int L_DUMMY = L_PROG + 64;
constexpr int L_END = L_DUMMY + 256;
static_assert(L_END <= 163840, "LDS");
constexpr int PFW = 1;

DI unsigned pack_bf16(float a, float b) {
  f32x2 v = {a, b};
  bf16x2_t r = __builtin_convertvector(v, bf16x2_t);
  return __builtin_bit_cast(unsigned, r);
}
DI f32x16 mfma32(bf16x8 a, bf16x8 b, f32x16 c) { return __builtin_amdgcn_mfma_f32_32x32x16_bf16(a, b, c, 0, 0, 0); }
DI bf16x8 as_frag(u32x4 v) { return __builtin_bit_cast(bf16x8, v); }
DI s16x4 lds_tr(unsigned a) {
  return __builtin_bit_cast(s16x4, __builtin_amdgcn_ds_read_tr16_b64_v4i16((__attribute__((address_space(3))) s16x4*)(size_t)a));
}

DI void glds16(unsigned voff, const void* gbase, unsigned ldsaddr) {
  asm volatile("s_mov_b32 m0, %2\n\ts_nop 3\n\tglobal_load_lds_dwordx4 %0, %1" ::"v"(voff), "s"(gbase), "s"(ldsaddr)
               : "memory");
}
DI void glds4(unsigned voff, const void* gbase) {
  asm volatile("global_load_lds_dword %0, %1" ::"v"(voff), "s"(gbase) : "memory");
}
DI int ld4_async(const void* gbase, unsigned voff) {
  int r;
  asm volatile("global_load_dword %0, %1, %2" : "=v"(r) : "v"(voff), "s"(gbase) : "memory");
  return r;
}
DI int ld2_async(const void* gbase, unsigned voff) {
  int r;
  asm volatile("s_nop 4\n\tglobal_load_ushort %0, %1, %2" : "=v"(r) : "v"(voff), "s"(gbase) : "memory");
  return r;
}
DI u32x4 ld16_async(const void* gbase, unsigned voff) {
  u32x4 r;
  asm volatile("global_load_dwordx4 %0, %1, %2" : "=v"(r) : "v"(voff), "s"(gbase) : "memory");
  return r;
}
DI u32x4 ld16_masked(const void* gbase, unsigned voff, unsigned r, unsigned len) {
  u32x4 d;
  unsigned long long sv;
  asm volatile(
      "v_cmp_lt_u32 vcc, %[r], %[len]\n\t"
      "s_and_saveexec_b64 %[sv], vcc\n\t"
      "global_load_dwordx4 %[d], %[off], %[base]\n\t"
      "s_mov_b64 exec, %[sv]"
      : [d] "=v"(d), [sv] "=&s"(sv)
      : [r] "v"(r), [len] "v"(len), [off] "v"(voff), [base] "s"(gbase)
      : "memory", "vcc", "scc");
  return d;
}
DI void tie4(u32x4& v) { asm volatile("" : "+v"(v)); }
DI void tie(int& v) { asm volatile("" : "+v"(v)); }
DI void st16_wt(void* gbase, unsigned voff, u32x4 v) {
  __builtin_amdgcn_raw_buffer_store_b128(v, __builtin_amdgcn_make_buffer_rsrc(gbase, 0, 0x2000000, 0x00020000), voff, 0, 16);
}
DI int fresh_lane() {
  int l;
  asm volatile("v_mbcnt_lo_u32_b32 %0, -1, 0\n\tv_mbcnt_hi_u32_b32 %0, -1, %0" : "=v"(l));
  return l;
}
DI void dma_tile(int tt, int nt, unsigned xd, unsigned ed, unsigned a0, unsigned a1, unsigned a2, unsigned a3, unsigned ae,
                 const uint16_t* x, const char* ep) {
  asm volatile(
      "s_cmp_ge_i32 %[tt], %[nt]\n\t"
      "s_cbranch_scc1 .Lskip%=\n\t"
      "s_mov_b32 m0, %[xd]\n\t"
      "s_nop 1\n\t"
      "global_load_lds_dwordx4 %[a0], %[x0]\n\t"
      "global_load_lds_dwordx4 %[a1], %[x1] offset:1024\n\t"
      "global_load_lds_dwordx4 %[a2], %[x2] offset:2048\n\t"
      "global_load_lds_dwordx4 %[a3], %[x3] offset:3072\n\t"
      "s_mov_b32 m0, %[ed]\n\t"
      "s_nop 0\n\t"
      "global_load_lds_dwordx4 %[ae], %[ep]\n"
      ".Lskip%=:"
      :
      : [tt] "s"(tt), [nt] "s"(nt), [xd] "s"(xd), [ed] "s"(ed), [a0] "v"(a0), [a1] "v"(a1), [a2] "v"(a2), [a3] "v"(a3),
        [ae] "v"(ae), [x0] "s"(x), [x1] "s"(x - 512), [x2] "s"(x - 1024), [x3] "s"(x - 1536), [ep] "s"(ep)
      : "memory", "scc");
}
template <int OFF> DI void dma_piece(int tt, int nt, unsigned ld, unsigned a, const char* gp) {
  asm volatile(
      "s_cmp_ge_i32 %[tt], %[nt]\n\t"
      "s_cbranch_scc1 .Lskip%=\n\t"
      "s_mov_b32 m0, %[ld]\n\t"
      "s_nop 0\n\t"
      "global_load_lds_dwordx4 %[a], %[gp] offset:%[off]\n"
      ".Lskip%=:"
      :
      : [tt] "s"(tt), [nt] "s"(nt), [ld] "s"(ld), [a] "v"(a), [gp] "s"(gp), [off] "n"(OFF)
      : "memory", "scc");
}
DI void own_rows(int tt, int nt, u32x4& so, unsigned& eo, int node0, int nlast) {
  unsigned a0 = so[0], a1 = so[1], a2 = so[2], a3 = so[3];
  asm volatile(
      "s_cmp_lg_i32 %[tt], %[nt]\n\t"
      "s_cbranch_scc1 .Lown%=\n\t"
      "v_mbcnt_lo_u32_b32 %[e], -1, 0\n\t"
      "v_mbcnt_hi_u32_b32 %[e], -1, %[e]\n\t"
      "v_lshrrev_b32 %[e], 3, %[e]\n\t"
      "v_add_u32 %[a0], %[n0], %[e]\n\t"
      "v_add_u32 %[a1], 8, %[a0]\n\t"
      "v_add_u32 %[a2], 16, %[a0]\n\t"
      "v_add_u32 %[a3], 24, %[a0]\n\t"
      "v_min_i32 %[a0], %[nl], %[a0]\n\t"
      "v_min_i32 %[a1], %[nl], %[a1]\n\t"
      "v_min_i32 %[a2], %[nl], %[a2]\n\t"
      "v_min_i32 %[a3], %[nl], %[a3]\n\t"
      "v_lshlrev_b32 %[a0], 7, %[a0]\n\t"
      "v_lshlrev_b32 %[a1], 7, %[a1]\n\t"
      "v_lshlrev_b32 %[a2], 7, %[a2]\n\t"
      "v_lshlrev_b32 %[a3], 7, %[a3]\n\t"
      "v_mov_b32 %[e], 0\n"
      ".Lown%=:"
      : [a0] "+v"(a0), [a1] "+v"(a1), [a2] "+v"(a2), [a3] "+v"(a3), [e] "+v"(eo)
      : [tt] "s"(tt), [nt] "s"(nt), [n0] "s"(node0), [nl] "s"(nlast)
      : "scc");
  so = u32x4{a0, a1, a2, a3};
}
DI void wait_oldest(int tt, int nt) {
  asm volatile(
      "s_cmp_lt_i32 %0, %1\n\t"
      "s_cbranch_scc1 .Lw%=\n\t"
      "s_waitcnt vmcnt(0)\n"
      ".Lw%=:\n\t"
      "s_waitcnt vmcnt(5)" ::"s"(tt), "s"(nt)
      : "memory", "scc");
}
template <int N> DI void wait_vm() { asm volatile("s_waitcnt vmcnt(%0)" ::"n"(N) : "memory"); }

template <int CTRL> DI unsigned dpp_add(unsigned v) {
  return v + (unsigned)__builtin_amdgcn_update_dpp(0, (int)v, CTRL, 0xf, 0xf, true);
}
DI unsigned wave_incl_scan(unsigned v) {
  v = dpp_add<0x111>(v);
  v = dpp_add<0x112>(v);
  v = dpp_add<0x114>(v);
  v = dpp_add<0x118>(v);
  v = v + (unsigned)__builtin_amdgcn_update_dpp(0, (int)v, 0x142, 0xa, 0xf, false);
  v = v + (unsigned)__builtin_amdgcn_update_dpp(0, (int)v, 0x143, 0xc, 0xf, false);
  return v;
}

constexpr int K1_RECS = 0;
constexpr int K1_HIST = K1_RECS + CSM * 8;
constexpr int K1_WSUM = K1_HIST + NBIN * 4;
constexpr int K1_END = K1_WSUM + 64;
constexpr int EPT = 4;
constexpr int EW = (CSM / EPT + 63) / 64;
constexpr int BPT = 8;
constexpr int SW0 = K1T / 64 - NBIN / BPT / 64;
static_assert(SUB == 1 && NPH == 1 && NBIN / BPT == 256 && EW <= 15, "k1 shape");
constexpr int OPT = (CSM / 2 + K1T - 1) / K1T;

extern "C" __global__ __launch_bounds__(K1T) void k1_partition(const int* __restrict__ dst, const int* __restrict__ src,
                                                               char* __restrict__ ws, int E, int CS, unsigned N,
                                                               unsigned magic, const uint16_t* __restrict__ wa,
                                                               const uint16_t* __restrict__ wb, const uint16_t* __restrict__ wc,
                                                               const uint16_t* __restrict__ wu,
                                                               const uint16_t* __restrict__ wv) {
  __shared__ __attribute__((aligned(16))) char smem[K1_END];
  u32x2* recs_s = (u32x2*)(smem + K1_RECS);
  unsigned* hist = (unsigned*)(smem + K1_HIST);
  unsigned* wsum = (unsigned*)(smem + K1_WSUM);
  const int tid = threadIdx.x, lane = tid & 63;
  const int wave = __builtin_amdgcn_readfirstlane(tid >> 6);
  asm volatile("" ::"s"(dst), "s"(src), "s"(ws), "s"(E), "s"(CS), "s"(N), "s"(magic));
  const int base = blockIdx.x * CS;
  const int e0 = base + EPT * tid;
  const int ea = min(e0, E - EPT);
  const int elim = min(base + CS, E);
  const bool ejob = wave < EW;
  u32x4 d, s;
  if (ejob) d = ld16_async(dst, (unsigned)ea * 4u);
  SB();
  const bool wjob = blockIdx.x < 34 && wave == 15;
  if (wave >= 8) {
    static_assert(NBIN / 4 == K1T / 2, "zeroing shape");
    *(u32x4*)(hist + (tid - K1T / 2) * 4) = u32x4{0, 0, 0, 0};
  }
  __syncthreads();
  int wj[8];
  if (wjob) {
    const int f = blockIdx.x, c = lane & 31, h = lane >> 5;
    const int m = f >> 3, sk = f < 32 ? (f >> 1) & 3 : 0, n = f & 1;
    const uint16_t* w = m == 0 ? wa : m == 1 ? wb : m == 2 ? wv : m == 3 ? wu : wc;
#pragma unroll
    for (int j = 0; j < 8; ++j) wj[j] = ld2_async(w, (unsigned)((16 * sk + 8 * h + j) * 64 + 2 * c + n) * 2u);
  }
  unsigned key[EPT], rank[EPT];
  u32x2 rec[EPT];
  bool ok[EPT];
  if (ejob) {
    wait_vm<0>();
    s = ld16_async(src, (unsigned)ea * 4u);
    tie4(d);
#pragma unroll
    for (int j = 0; j < EPT; ++j) {
      const int e = ea + j;
      ok[j] = e >= e0 && e < elim;
      const unsigned dd = d[j];
      const unsigned gg = __umulhi(dd, magic);
      const unsigned sl = dd - GSTART(gg, N);
      key[j] = ok[j] ? gg : 0u;
      rec[j] = u32x2{sl << 1, ((unsigned)e << 5) | (gg << 29)};
      if (ok[j]) rank[j] = atomicAdd(&hist[key[j]], 1u);
    }
  }
  __syncthreads();
  unsigned cb[BPT], inc, tot;
  const int bk = tid - SW0 * 64;
  if (wave >= SW0) {
#pragma unroll
    for (int i = 0; i < BPT / 4; ++i) {
      const u32x4 v = *(const u32x4*)(hist + bk * BPT + 4 * i);
      cb[4 * i] = v[0], cb[4 * i + 1] = v[1], cb[4 * i + 2] = v[2], cb[4 * i + 3] = v[3];
    }
    tot = 0;
#pragma unroll
    for (int i = 0; i < BPT; ++i) tot += cb[i];
    inc = wave_incl_scan(tot);
    if (lane == 63) wsum[wave - SW0] = inc;
  }
  __syncthreads();
  if (wave >= SW0) {
    unsigned pre = inc - tot;
    {
      u32x4 ws4 = *(const u32x4*)wsum;
      tie4(ws4);
#pragma unroll
      for (int w = 0; w < 3; ++w) pre += (w < wave - SW0) ? ws4[w] : 0u;
    }
    const unsigned st = pre;
    unsigned pb[BPT];
#pragma unroll
    for (int q = 0; q < BPT; ++q) {
      pb[q] = pre;
      pre += cb[q];
    }
#pragma unroll
    for (int i = 0; i < BPT / 4; ++i) {
      u32x4 v = {pb[4 * i], pb[4 * i + 1], pb[4 * i + 2], pb[4 * i + 3]};
      *(u32x4*)(hist + bk * BPT + 4 * i) = v;
    }
    u32x4 t;
    t[0] = cb[0] | ((st & 15u) << 12) | (cb[1] << 16) | (((st >> 4) & 15u) << 28);
    t[1] = cb[2] | ((st >> 8) << 12) | (cb[3] << 16);
    t[2] = cb[4] | (cb[5] << 16);
    t[3] = cb[6] | (cb[7] << 16);
    SB();
    st16_wt(ws, (unsigned)WS_TAB + (unsigned)TABIDX(bk, blockIdx.x) * 16u, t);
  }
  __syncthreads();
  if (ejob) {
    int hb[EPT];
#pragma unroll
    for (int j = 0; j < EPT; ++j) hb[j] = (int)hist[key[j]];
#pragma unroll
    for (int j = 0; j < EPT; ++j) tie(hb[j]);
    if (wave >= SW0) wait_vm<1>(); else wait_vm<0>();
    tie4(s);
#pragma unroll
    for (int j = 0; j < EPT; ++j) {
      rec[j][0] |= s[j] << 7;
      if (ok[j]) recs_s[(unsigned)hb[j] + rank[j]] = rec[j];
    }
  }
  if (wjob) {
    u32x4 v;
    wait_vm<1>();
#pragma unroll
    for (int j = 0; j < 4; ++j) {
      tie(wj[2 * j]);
      tie(wj[2 * j + 1]);
      v[j] = (unsigned)wj[2 * j] | ((unsigned)wj[2 * j + 1] << 16);
      if ((blockIdx.x >> 3) == 1 || blockIdx.x >= 32)
        v[j] = pack_bf16(__uint_as_float((unsigned)wj[2 * j] << 16) * NLOG2E, __uint_as_float((unsigned)wj[2 * j + 1] << 16) * NLOG2E);
    }
    st16_wt(ws, (unsigned)WS_WFRAG + (unsigned)(blockIdx.x * 64 + lane) * 16u, v);
  }
  __syncthreads();
  {
    u32x4 v[OPT];
#pragma unroll
    for (int j = 0; j < OPT; ++j) v[j] = *(const u32x4*)(smem + K1_RECS + min(tid + j * K1T, CSM / 2 - 1) * 16);
#pragma unroll
    for (int j = 0; j < OPT; ++j) tie4(v[j]);
#pragma unroll
    for (int j = 0; j < OPT; ++j)
      if (tid + j * K1T < CSM / 2) st16_wt(ws, (unsigned)WS_RECS + (unsigned)(blockIdx.x * (CSM / 2) + tid + j * K1T) * 16u, v[j]);
  }
}

extern "C" __global__ __launch_bounds__(K2T, 2) void k2_main(const char* __restrict__ ws, const uint16_t* __restrict__ x,
                                                             const char* __restrict__ eattr, uint16_t* __restrict__ out,
                                                             int N) {
  const u32x2* __restrict__ recs = (const u32x2*)(ws + WS_RECS);
  __shared__ __attribute__((aligned(16))) char smem[L_END];
  const int wave = __builtin_amdgcn_readfirstlane((int)threadIdx.x >> 6);
  const int b = (blockIdx.x & 7) * (NB / 8) + (blockIdx.x >> 3);
  const unsigned sbase = (unsigned)(size_t)smem;
  u32x2* l_list = (u32x2*)(smem + L_LIST);
  unsigned* l_scan = (unsigned*)(smem + L_SCAN);
  unsigned* l_misc = (unsigned*)(smem + L_MISC);
  u32x4 te[SPT];
#pragma unroll
  for (int i = 0; i < SPT; ++i) te[i] = ld16_async(ws, (unsigned)WS_TAB + (unsigned)TABIDX(b, ((int)threadIdx.x / TPS) * SPT + i) * 16u);
  SB();
  int grp = -1;

  f32x16 den[2], num[2];
#pragma unroll
  for (int n = 0; n < 2; ++n)
#pragma unroll
    for (int i = 0; i < 16; ++i) den[n][i] = 1e-6f, num[n][i] = 0.f;

  const unsigned img = sbase + L_IMG + wave * 2048;
  bf16x8 X[4];
  int p0 = 0, p1 = NSEG;
  int nt = 0;
  for (;;) {
    const int lane = fresh_lane();
    const int tid = wave * 64 + lane;
    unsigned zr;
    asm volatile("v_mov_b32 %0, 0" : "=v"(zr));
    const int par = tid % TPS;
    {
      u32x4 z = {zr, zr, zr, zr};
      *(u32x4*)(smem + L_IMG + tid * 32) = z;
      *(u32x4*)(smem + L_IMG + tid * 32 + 16) = z;
    }
    SB();
    wait_vm<0>();
#pragma unroll
    for (int i = 0; i < SPT; ++i) tie4(te[i]);
    SB();
    unsigned st[SPT], ln[SPT];
    u32x4 rq[SPT][RPL], tc[SPT];
#pragma unroll
    for (int i = 0; i < SPT; ++i) {
      const u32x4 t = te[i];
      st[i] = ((t[0] >> 12) & 15u) | ((t[0] >> 28) << 4) | (((t[1] >> 12) & 15u) << 8);
#pragma unroll
      for (int j = 0; j < 4; ++j) tc[i][j] = t[j] & 0x0FFF0FFFu;
      const unsigned a = tc[i][0] + tc[i][1] + tc[i][2] + tc[i][3];
      const unsigned len = (a & 0xFFFFu) + (a >> 16);
      st[i] += (unsigned)(((tid / TPS) * SPT + i) % NQ) * CSM;
#pragma unroll
      for (int q = 0; q < RPL; ++q) {
        const unsigned r = 2u * (q * TPS + par);
        rq[i][q] = ld16_masked(recs, (st[i] + r) * 8u, r, len);
      }
    }
    SB();
    bf16x8 WB[4][2], WV[4][2], AB[2][2];
    unsigned pre[5], tot[4];
    u32x4 cw[SPT];
    bool again, wdone = false;
    do {
#pragma unroll
    for (int i = 0; i < SPT; ++i) {
      const int sg = (tid / TPS) * SPT + i;
      const bool in = sg >= p0 && sg < p1;
#pragma unroll
      for (int j = 0; j < 4; ++j) cw[i][j] = in ? tc[i][j] : 0u;
      const unsigned a = cw[i][0] + cw[i][1] + cw[i][2] + cw[i][3];
      ln[i] = (a & 0xFFFFu) + (a >> 16);
    }
    unsigned sv[5], si[5];
#pragma unroll
    for (int j = 0; j < 4; ++j) {
      sv[j] = cw[0][j];
#pragma unroll
      for (int i = 1; i < SPT; ++i) sv[j] += cw[i][j];
    }
    sv[4] = ln[0];
#pragma unroll
    for (int i = 1; i < SPT; ++i) sv[4] += ln[i];
#pragma unroll
    for (int j = 0; j < 5; ++j) si[j] = wave_incl_scan(par == 0 ? sv[j] : 0u);
    if (lane == 63) {
#pragma unroll
      for (int j = 0; j < 5; ++j) l_scan[wave * 8 + j] = si[j];
    }
    if (tid == 0) l_misc[zr] = zr;
    __syncthreads();
    if (!wdone) {
      wdone = true;
#pragma unroll
      for (int i = 0; i < 4; ++i)
        glds16((unsigned)WS_WFRAG + (unsigned)((wave * 4 + i) * 1024 + lane * 16), ws, sbase + L_WA + (wave * 4 + i) * 1024);
      glds16((unsigned)WS_WFRAG + (unsigned)(32 * 1024 + (wave & 1) * 1024 + lane * 16), ws, sbase + L_WC + (wave & 1) * 1024);
    }
    {
      const u32x4 v = *(const u32x4*)(l_scan + (lane & 7) * 8);
      unsigned u[5] = {v[0], v[1], v[2], v[3], l_scan[(lane & 7) * 8 + 4]};
#pragma unroll
      for (int j = 0; j < 5; ++j) {
        const unsigned inc = dpp_add<0x114>(dpp_add<0x112>(dpp_add<0x111>(u[j])));
        const unsigned t = __builtin_amdgcn_readlane(inc, 7);
        pre[j] = si[j] - sv[j] + __builtin_amdgcn_readlane(inc - u[j], wave);
        if (j < 4) tot[j] = t;
        else again = t > (unsigned)LIM;
      }
    }
    if (again) {
      unsigned c = 0, pp = pre[4];
#pragma unroll
      for (int i = 0; i < SPT; ++i) {
        const int sg = (tid / TPS) * SPT + i;
        pp += ln[i];
        c += (par == 0 && sg >= p0 && sg < p1 && pp <= (unsigned)LIM) ? 1u : 0u;
      }
      if (c) atomicAdd(&l_misc[zr], c);
      __syncthreads();
      p1 = p0 + (int)__builtin_amdgcn_readfirstlane(l_misc[zr]);
      __syncthreads();
    }
    } while (again);
    if (grp < 0) {
      const unsigned t01 = (lane & 2) ? tot[1] : tot[0], t23 = (lane & 2) ? tot[3] : tot[2];
      const unsigned tq = (lane & 4) ? t23 : t01;
      const unsigned mine = (((tq >> ((lane & 1) * 16)) & 0xFFFFu) << 3) | (unsigned)(lane & 7);
      unsigned rank = 0;
      rank += (unsigned)__builtin_amdgcn_update_dpp(0, (int)mine, 0x121, 0xf, 0xf, false) > mine ? 1u : 0u;
      rank += (unsigned)__builtin_amdgcn_update_dpp(0, (int)mine, 0x122, 0xf, 0xf, false) > mine ? 1u : 0u;
      rank += (unsigned)__builtin_amdgcn_update_dpp(0, (int)mine, 0x123, 0xf, 0xf, false) > mine ? 1u : 0u;
      rank += (unsigned)__builtin_amdgcn_update_dpp(0, (int)mine, 0x124, 0xf, 0xf, false) > mine ? 1u : 0u;
      rank += (unsigned)__builtin_amdgcn_update_dpp(0, (int)mine, 0x125, 0xf, 0xf, false) > mine ? 1u : 0u;
      rank += (unsigned)__builtin_amdgcn_update_dpp(0, (int)mine, 0x126, 0xf, 0xf, false) > mine ? 1u : 0u;
      rank += (unsigned)__builtin_amdgcn_update_dpp(0, (int)mine, 0x127, 0xf, 0xf, false) > mine ? 1u : 0u;
      const unsigned want = wave < 4 ? wave : 11 - wave;
      grp = __builtin_ctzll(__builtin_amdgcn_ballot_w64(rank == want));
    }
#pragma unroll
    for (int i = 0; i < 4; ++i) {
      const int r = 8 * i + (lane >> 3);
      const unsigned nd = (unsigned)min((int)GSTART(b * NG + grp, N) + r, N - 1);
      glds16(nd * 128u + (unsigned)(((lane & 7) ^ ((r >> 1) & 7)) * 16), x, sbase + L_XB + wave * 8192 + 4096 + i * 1024);
    }
    unsigned gbase = 0, gt = 0;
    {
      unsigned run = 0;
#pragma unroll
      for (int j = 0; j < 4; ++j) {
        const unsigned t0 = tot[j] & 0xFFFFu, t1 = tot[j] >> 16;
        const unsigned r1 = run + ((t0 + 31u) & ~31u);
        if (2 * j == grp) gbase = run, gt = t0;
        if (2 * j + 1 == grp) gbase = r1, gt = t1;
        pre[j] += run | (r1 << 16);
        run = r1 + ((t1 + 31u) & ~31u);
      }
    }
    nt = (int)((gt + 31u) >> 5);
    *(int*)(smem + L_PROG + wave * 4) = nt;
    *(int*)(smem + L_PROG + wave * 4 + 32) = nt;
    if (wave < PFW) wait_vm<4>(); else wait_vm<9>();
#pragma unroll
    for (int i = 0; i < SPT; ++i)
#pragma unroll
      for (int q = 0; q < RPL; ++q) tie4(rq[i][q]);
    if (wave < PFW) {
      asm volatile("s_mov_b32 m0, %0\n\ts_nop 1" ::"s"(sbase + L_DUMMY) : "memory");
#pragma unroll
      for (int q = 0; q < RPL; ++q) {
        const unsigned r = 2u * (q * TPS + par);
        if (__builtin_amdgcn_ballot_w64(ln[0] > r) == 0) break;
#pragma unroll
        for (int j = 0; j < 2; ++j)
          if (ln[0] > r + j) glds4(rq[0][q][2 * j + 1] & 0x1FFFFFE0u, eattr);
      }
    }
    {
#pragma unroll
      for (int i = 0; i < SPT; ++i) {
        unsigned dl[4];
        unsigned run = 0;
#pragma unroll
        for (int j = 0; j < 4; ++j) {
          const unsigned c0 = cw[i][j] & 0xFFFFu, c1 = cw[i][j] >> 16;
          dl[j] = (pre[j] << 3) + (unsigned)L_LIST * 0x10001u - ((run | ((run + c0) << 16)) << 3);
          run += c0 + c1;
          pre[j] += cw[i][j];
        }
        auto put = [&](unsigned w0, unsigned w1, unsigned idx) {
          const bool up = (int)w1 < 0;
          const unsigned lo = up ? dl[2] : dl[0], hi = up ? dl[3] : dl[1];
          const unsigned sel = ((w1 >> 29) & 3u) * 0x0202u + (zr | 0x0c0c0100u);
          const unsigned ad = __builtin_amdgcn_perm(hi, lo, sel);
          *(u32x2*)(smem + ad + idx * 8u) = u32x2{w0, w1};
        };
#pragma unroll
        for (int q = 0; q < RPL; ++q) {
          const unsigned r = 2u * (q * TPS + par);
          if (__builtin_amdgcn_ballot_w64(ln[i] > r) == 0) break;
#pragma unroll
          for (int j = 0; j < 2; ++j)
            if (ln[i] > r + j) put(rq[i][q][2 * j], rq[i][q][2 * j + 1], r + j);
        }
        for (unsigned r = 2 * RPL * TPS + par; r < ln[i]; r += TPS) {
          const u32x2 rc = recs[st[i] + r];
          put(rc[0], rc[1], r);
        }
      }
      const unsigned pos = gbase + gt + lane;
      if (pos < gbase + (unsigned)nt * 32u) l_list[pos] = u32x2{zr | 62u, zr};
    }
    if (wave >= PFW) wait_vm<4>();
    __syncthreads();

    {
      const int lane = fresh_lane();
      const int c = lane & 31, h = lane >> 5;
      const unsigned xb = sbase + L_XB + wave * 8192;
      const unsigned eb = sbase + L_EB + wave * 2048;
      unsigned xsw[2];
#pragma unroll
      for (int i = 0; i < 2; ++i) xsw[i] = (unsigned)(((lane & 7) ^ ((4 * i + (lane >> 4)) & 7)) * 16);
      const unsigned esw = ((lane ^ (lane >> 4)) & 1) * 16;
      const unsigned ohw = (unsigned)(L_IMG + wave * 2048 + c * 64 + (((c >> 2) & 3) << 4));
      unsigned xrd[4];
#pragma unroll
      for (int s = 0; s < 4; ++s) xrd[s] = (unsigned)(L_XB + wave * 8192 + c * 128 + (((2 * s + h) ^ ((c >> 1) & 7)) * 16));
      unsigned erd = (unsigned)(L_EB + wave * 2048 + c * 32 + ((h ^ ((c >> 3) & 1)) * 16));
      const unsigned ohr0 = (unsigned)(L_IMG + wave * 2048 + c * 64 + (((0 + h) ^ ((c >> 2) & 3)) * 16));
      const unsigned ohr1 = (unsigned)(L_IMG + wave * 2048 + c * 64 + (((2 + h) ^ ((c >> 2) & 3)) * 16));
      unsigned trd[2];
      {
        const int q = (lane >> 2) & 3, p = lane & 3, cb = (lane >> 4) & 1;
#pragma unroll
        for (int jh = 0; jh < 2; ++jh)
          trd[jh] = img + (unsigned)((8 * jh + 4 * h + q) * 64 + (((2 * cb + (p >> 1)) ^ ((2 * jh + h) & 3)) * 16) + 8 * (p & 1));
      }
      asm volatile("" : "+v"(xrd[0]), "+v"(xrd[1]), "+v"(xrd[2]), "+v"(xrd[3]), "+v"(erd));
      unsigned wvr = (unsigned)(L_WV + lane * 16);
      asm volatile("" : "+v"(wvr));
      unsigned lsrc = L_LIST + (gbase + (unsigned)(lane >> 3)) * 8u;
      unsigned leof = L_LIST + (gbase + (unsigned)(lane >> 1)) * 8u + 4u;
      unsigned loha = L_LIST + (gbase + (unsigned)c) * 8u;
      asm volatile("" : "+v"(lsrc), "+v"(leof), "+v"(loha));

      bf16x8 EF, OH0, OH1, ST0, ST1, WC;
      const int ntd = nt + 1;
      const int node0 = (int)GSTART(b * NG + grp, N);
      f32x16 G[2], HV;
      const f32x16 zero16 = {0.f, 0.f, 0.f, 0.f, 0.f, 0.f, 0.f, 0.f, 0.f, 0.f, 0.f, 0.f, 0.f, 0.f, 0.f, 0.f};
      unsigned oha;
      u32x4 so;
      unsigned eo;

      auto dma = [&](int tt, auto BUFC) {
        constexpr int BUF = decltype(BUFC)::value;
        dma_tile(tt, ntd, xb + BUF * 4096u, eb + BUF * 1024u, (so[0] & ~127u) | xsw[0], (so[1] & ~127u) | xsw[1],
                 (so[2] & ~127u) | xsw[0], (so[3] & ~127u) | xsw[1], (eo & 0x1FFFFFE0u) | esw, x, eattr);
      };
      auto dmap = [&](int tt, auto BUFC, auto PC) {
        constexpr int BUF = decltype(BUFC)::value, P = decltype(PC)::value;
        if (P < 4)
          dma_piece<P * 1024>(tt, ntd, xb + BUF * 4096u, (so[P & 3] & ~127u) | xsw[P & 1], (const char*)(x - 512 * P));
        else
          dma_piece<0>(tt, ntd, eb + BUF * 1024u, (eo & 0x1FFFFFE0u) | esw, eattr);
      };
      auto own = [&](int tt) { own_rows(tt, nt, so, eo, node0, N - 1); };
      auto wait_tile = [&](int tt) { wait_oldest(tt, ntd); };
      auto rd_list = [&](int t) {
#pragma unroll
        for (int i = 0; i < 4; ++i) so[i] = *(const unsigned*)(smem + lsrc + t * 256 + i * 64);
        eo = *(const unsigned*)(smem + leof + t * 256);
      };
      unsigned ohr;
      int prem, rems;
      unsigned pga;
      asm volatile("v_mov_b32 %0, %1" : "=v"(pga) : "s"(L_PROG + wave * 4));
      auto rd_oha = [&](int t) { ohr = *(const unsigned*)(smem + loha + t * 256); };
      auto set_oh = [&]() {
        oha = (ohr & 62u) ^ ohw;
        *(uint16_t*)(smem + oha) = 0x3F80;
      };
      auto rd_x = [&](auto BUFC) {
        constexpr int BUF = decltype(BUFC)::value;
#pragma unroll
        for (int s = 0; s < 4; ++s) X[s] = as_frag(*(const u32x4*)(smem + xrd[s] + BUF * 4096));
      };
      auto rd_oh = [&](int n) {
        OH0 = as_frag(*(const u32x4*)(smem + ohr0));
        OH1 = as_frag(*(const u32x4*)(smem + ohr1));
      };
      auto rd_wc = [&](int n) {
        WC = as_frag(*(const u32x4*)(smem + wvr + (L_WC - L_WV) + n * 1024));
      };
      auto rd_e = [&](auto BUFC) {
        constexpr int BUF = decltype(BUFC)::value;
        EF = as_frag(*(const u32x4*)(smem + erd + BUF * 1024));
      };

      u32x4 pe0, pm0, pe1, pm1;
#define GE(s, i, j) G[n][8 * (s) + 2 * (i) + (j)]
#define EX(s, i) GE(s, i, 0) = __builtin_amdgcn_exp2f(GE(s, i, 0)); GE(s, i, 1) = __builtin_amdgcn_exp2f(GE(s, i, 1));
#define EQ(s, i) GE(s, i, 0) = __builtin_amdgcn_rcpf(GE(s, i, 0)); GE(s, i, 1) = __builtin_amdgcn_rcpf(GE(s, i, 1));
#define EP(s, i)                                                   \
  {                                                                \
    float p0_ = GE(s, i, 0) + 1.0f;                                \
    asm("" : "+v"(p0_));                                           \
    GE(s, i, 0) = p0_;                                             \
    GE(s, i, 1) = GE(s, i, 1) + 1.0f;                              \
  }
#define ET(s, i)                                                   \
  {                                                                \
    float m0_ = GE(s, i, 0) * HV[8 * (s) + 2 * (i)];               \
    float m1_ = GE(s, i, 1) * HV[8 * (s) + 2 * (i) + 1];           \
    asm("" : "+v"(m0_));                                           \
    pe##s[i] = pack_bf16(GE(s, i, 0), GE(s, i, 1));                \
    pm##s[i] = pack_bf16(m0_, m1_);                                \
  }

      auto stage = [&](int t, auto NC, auto BUFC, auto MODEC) {
        constexpr int n = decltype(NC)::value;
        constexpr int m = 1 - n;
        constexpr int BUF = decltype(BUFC)::value;
        constexpr int MODE = decltype(MODEC)::value;
        constexpr bool S1 = MODE != 2;
        constexpr bool PD = MODE == 0 || (MODE == 1 && n == 1);
        constexpr bool GN = MODE == 0 || n == 0;
        HV = mfma32(X[0], WV[0][n], zero16);
        EX(0, 0) EX(0, 1)
        SB();
        if (PD) den[m] = mfma32(ST1, as_frag(pe1), den[m]);
        EX(0, 2) EX(0, 3)
        if (n == 0 && MODE == 0) dmap(t + 2, IC<1 - BUF>{}, IC<0>{});
        SB();
        HV = mfma32(X[1], WV[1][n], HV);
        EP(0, 0) EP(0, 1)
        if (S1) { EX(1, 0) }
        SB();
        if (PD) num[m] = mfma32(ST1, as_frag(pm1), num[m]);
        if (GN) rd_oh(m);
        if (n == 0) {
          const s16x4 t00 = lds_tr(trd[0]), t01 = lds_tr(trd[1]);
          ST0 = __builtin_shufflevector(t00, t01, 0, 1, 2, 3, 4, 5, 6, 7);
          if (S1) {
            const s16x4 t10 = lds_tr(trd[0] + 1024), t11 = lds_tr(trd[1] + 1024);
            ST1 = __builtin_shufflevector(t10, t11, 0, 1, 2, 3, 4, 5, 6, 7);
          }
          *(uint16_t*)(smem + oha) = 0;
          if (MODE == 0) rd_oha(2 - BUF);
          asm volatile("s_sub_i32 %0, %1, %2" : "=s"(rems) : "s"(nt), "s"(t) : "scc");
          *(int*)(smem + pga) = rems;
          *(int*)(smem + pga + 32) = rems;
          prem = *(const int*)(smem + pga + 16);
        }
        EP(0, 2) EP(0, 3)
        if (S1) { EX(1, 1) }
        SB();
        HV = mfma32(X[2], WV[2][n], HV);
        EQ(0, 0) EQ(0, 1)
        SB();
        HV = mfma32(X[3], WV[3][n], HV);
        if (GN) rd_wc(m);
        if (n == 1) {
          if (MODE == 0) {
            wait_tile(t + 2);
            rd_e(BUFC);
            rd_x(BUFC);
          } else {
            wait_vm<0>();
#pragma unroll
            for (int s = 0; s < 4; ++s) {
              asm volatile("v_add_u32 %0, %1, %0" : "+v"(xrd[s]) : "s"(4096 - (t & 1) * 8192));
              X[s] = as_frag(*(const u32x4*)(smem + xrd[s]));
            }
          }
        }
        EQ(0, 2) EQ(0, 3)
        if (n == 0 && MODE == 0) dmap(t + 2, IC<1 - BUF>{}, IC<1>{});
        SB();
        if (GN) G[m] = mfma32(OH0, AB[0][m], zero16);
        if (n == 0) {
          if (MODE == 0) set_oh();
          int sp;
          asm volatile(
              "v_readfirstlane_b32 %0, %2\n\t"
              "s_setprio 0\n\t"
              "s_cmp_gt_i32 %1, %0\n\t"
              "s_cbranch_scc0 .Lp%=\n\t"
              "s_setprio 1\n"
              ".Lp%=:"
              : "=&s"(sp)
              : "s"(rems), "v"(prem)
              : "scc");
        }
        if (S1) { EX(1, 2) EX(1, 3) }
        SB();
        if (GN) G[m] = mfma32(OH1, AB[1][m], G[m]);
        ET(0, 0) ET(0, 1)
        SB();
        if (GN) G[m] = mfma32(EF, WC, G[m]);
        if (n == 1 && MODE == 0) rd_list(4 - BUF);
        ET(0, 2) ET(0, 3)
        SB();
        if (GN) G[m] = mfma32(X[0], WB[0][m], G[m]);
        if (S1) { EP(1, 0) EP(1, 1) EP(1, 2) EP(1, 3) }
        if (n == 0 && MODE == 0) dmap(t + 2, IC<1 - BUF>{}, IC<2>{});
        SB();
        if (GN) G[m] = mfma32(X[1], WB[1][m], G[m]);
        if (S1) { EQ(1, 0) EQ(1, 1) }
        SB();
        if (GN) G[m] = mfma32(X[2], WB[2][m], G[m]);
        if (S1) { EQ(1, 2) EQ(1, 3) }
        SB();
        if (GN) G[m] = mfma32(X[3], WB[3][m], G[m]);
        if (S1) { ET(1, 0) ET(1, 1) }
        if (n == 1 && MODE == 0) {
          own(t + 3);
          dmap(t + 3, BUFC, IC<4>{});
        }
        SB();
        den[n] = mfma32(ST0, as_frag(pe0), den[n]);
        if (S1) { ET(1, 2) ET(1, 3) }
        if (n == 0 && MODE == 0) dmap(t + 2, IC<1 - BUF>{}, IC<3>{});
        SB();
        num[n] = mfma32(ST0, as_frag(pm0), num[n]);
        SB();
      };

      rd_list(0);
      own(0);
      dma(0, IC<0>{});
      rd_list(1);
      rd_oha(0);
      set_oh();
      {
        unsigned war = (unsigned)(L_WA + lane * 16);
        asm volatile("" : "+v"(war));
        u32x4 wa[8];
#pragma unroll
        for (int k = 0; k < 8; ++k) wa[k] = *(const u32x4*)(smem + war + k * 1024);
        SB();
        wait_tile(0);
        u32x4 xa[4];
        const int ra = (c & 0x13) | ((c & 4) << 1) | ((c & 8) >> 1);
#pragma unroll
        for (int s = 0; s < 4; ++s)
          xa[s] = *(const u32x4*)(smem + L_XB + wave * 8192 + 4096 + ra * 128 + (((2 * s + h) ^ ((ra >> 1) & 7)) * 16));
        SB();
        f32x16 acc[2];
#pragma unroll
        for (int n = 0; n < 2; ++n)
#pragma unroll
          for (int i = 0; i < 16; ++i) acc[n][i] = 0.f;
#pragma unroll
        for (int s = 0; s < 4; ++s)
#pragma unroll
          for (int n = 0; n < 2; ++n) acc[n] = mfma32(as_frag(xa[s]), as_frag(wa[s * 2 + n]), acc[n]);
        SB();
        own(1);
        dma(1, IC<1>{});
        SB();
#pragma unroll
        for (int s = 0; s < 4; ++s)
#pragma unroll
          for (int n = 0; n < 2; ++n) WB[s][n] = as_frag(*(const u32x4*)(smem + war + (8 + s * 2 + n) * 1024));
#pragma unroll
        for (int s = 0; s < 4; ++s)
#pragma unroll
          for (int n = 0; n < 2; ++n) WV[s][n] = as_frag(*(const u32x4*)(smem + war + (16 + s * 2 + n) * 1024));
        rd_oh(0);
        rd_wc(0);
#pragma unroll
        for (int n = 0; n < 2; ++n)
#pragma unroll
          for (int s = 0; s < 2; ++s) {
            u32x4 p;
#pragma unroll
            for (int j = 0; j < 4; ++j) p[j] = pack_bf16(acc[n][8 * s + 2 * j] * NLOG2E, acc[n][8 * s + 2 * j + 1] * NLOG2E);
            tie4(p);
            AB[s][n] = as_frag(p);
          }
        G[0] = mfma32(OH0, AB[0][0], zero16);
        G[0] = mfma32(OH1, AB[1][0], G[0]);
        SB();
      }
      wait_tile(1);
      rd_x(IC<0>{});
      if (nt > 0) {
        rd_e(IC<0>{});
        rd_list(2);
        pe1 = u32x4{zr, zr, zr, zr};
        pm1 = pe1;
        ST1 = as_frag(pe1);
#pragma unroll
        for (int s = 0; s < 4; ++s) G[0] = mfma32(X[s], WB[s][0], G[0]);
        G[0] = mfma32(EF, WC, G[0]);
        SB();
        own(2);
        dmap(2, IC<0>{}, IC<4>{});
        int ntl = max(nt - 1, 1);
        asm volatile("" : "+s"(ntl));
        __builtin_assume(ntl > 0);
        for (int t = 0; t < ntl; t += 2) {
          stage(t, IC<0>{}, IC<1>{}, IC<0>{});
          stage(t, IC<1>{}, IC<1>{}, IC<0>{});
          if (t + 1 >= ntl) break;
          stage(t + 1, IC<0>{}, IC<0>{}, IC<0>{});
          stage(t + 1, IC<1>{}, IC<0>{}, IC<0>{});
          lsrc += 512u, leof += 512u, loha += 512u;
        }
        den[1] = mfma32(ST1, as_frag(pe1), den[1]);
        num[1] = mfma32(ST1, as_frag(pm1), num[1]);
        SB();
        auto rd_last = [&]() {
          asm volatile("v_add_u32 %0, %1, %0" : "+v"(erd) : "s"((ntl & 1) * 1024));
#pragma unroll
          for (int s = 0; s < 4; ++s) {
            asm volatile("v_add_u32 %0, %1, %0" : "+v"(xrd[s]) : "s"((ntl & 1) * 4096));
            X[s] = as_frag(*(const u32x4*)(smem + xrd[s]));
          }
          EF = as_frag(*(const u32x4*)(smem + erd));
          SB();
        };
        int lm = ntl == nt ? 0 : gt - (unsigned)ntl * 32u > 16u ? 1 : 2;
        asm volatile("" : "+s"(lm));
        if (lm == 1) {
          rd_last();
          stage(ntl, IC<0>{}, IC<0>{}, IC<1>{});
          stage(ntl, IC<1>{}, IC<0>{}, IC<1>{});
          den[1] = mfma32(ST1, as_frag(pe1), den[1]);
          num[1] = mfma32(ST1, as_frag(pm1), num[1]);
          SB();
        }
        asm volatile("" : "+s"(lm));
        if (lm == 2) {
          rd_last();
          stage(ntl, IC<0>{}, IC<0>{}, IC<2>{});
          stage(ntl, IC<1>{}, IC<0>{}, IC<2>{});
        }
      }
      *(uint16_t*)(smem + oha) = 0;
      *(int*)(smem + pga) = 0;
      *(int*)(smem + pga + 32) = 0;
      asm volatile("s_setprio 0");
    }
    if (p1 == NSEG) break;
    p0 = p1;
    p1 = NSEG;
    {
      const int tid2 = wave * 64 + fresh_lane();
#pragma unroll
      for (int i = 0; i < SPT; ++i) te[i] = ld16_async(ws, (unsigned)WS_TAB + (unsigned)TABIDX(b, (tid2 / TPS) * SPT + i) * 16u);
    }
    __builtin_amdgcn_s_waitcnt(0x0f70);
    __syncthreads();
  }

  {
    const int lane = fresh_lane();
    const int c = lane & 31, h = lane >> 5;
    const int node0 = (int)GSTART(b * NG + grp, N);
    const int nn = (int)GSTART(b * NG + grp + 1, N) - node0;
    const char* xr = smem + L_XB + wave * 8192 + (nt & 1) * 4096;
    char* ob = smem + L_XB + wave * 8192 + ((nt & 1) ^ 1) * 4096;
    bf16x8 WU[8];
#pragma unroll
    for (int k = 0; k < 8; ++k) WU[k] = as_frag(*(const u32x4*)(smem + L_WU + k * 1024 + lane * 16));
    unsigned xv[16];
#pragma unroll
    for (int i = 0; i < 16; ++i) {
      const int sw = ((i >> 1) & 1) + ((i >> 2) & 1) * 4;
      xv[i] = *(const unsigned*)(xr + h * 512 + (((c >> 2) ^ (sw + 2 * h)) * 16) + (c & 3) * 4 + ((i & 3) + 8 * (i >> 2)) * 128);
    }
    SB();
    f32x16 u[2];
#pragma unroll
    for (int n = 0; n < 2; ++n)
#pragma unroll
      for (int i = 0; i < 16; ++i) u[n][i] = 0.f;
    f32x2 xx[2][8];
#pragma unroll
    for (int k = 0; k < 8; ++k) {
      u[k & 1] = mfma32(X[k >> 1], WU[k], u[k & 1]);
      SB();
#pragma unroll
      for (int i = 4 * (k & 3); i < 4 * (k & 3) + 4; ++i) den[k >> 2][i] = __builtin_amdgcn_rcpf(den[k >> 2][i]);
      xx[0][k] = f32x2{__uint_as_float(xv[2 * k] << 16), __uint_as_float(xv[2 * k + 1] << 16)};
      xx[1][k] = f32x2{__uint_as_float(xv[2 * k] & 0xFFFF0000u), __uint_as_float(xv[2 * k + 1] & 0xFFFF0000u)};
      asm volatile("" : "+v"(xx[0][k]), "+v"(xx[1][k]));
      SB();
    }
    const f32x2 inf2 = {__builtin_inff(), __builtin_inff()};
    f32x2 z[2][8], t[2][8];
#pragma unroll
    for (int n = 0; n < 2; ++n)
#pragma unroll
      for (int j = 0; j < 8; ++j)
        z[n][j] = __builtin_elementwise_fma(f32x2{num[n][2 * j], num[n][2 * j + 1]}, f32x2{den[n][2 * j], den[n][2 * j + 1]},
                                            f32x2{u[n][2 * j], u[n][2 * j + 1]});
    SB();
#pragma unroll
    for (int j = 0; j < 8; ++j)
#pragma unroll
      for (int n = 0; n < 2; ++n) asm volatile("v_pk_mul_f32 %0, %1, %2 clamp" : "=v"(t[n][j]) : "v"(z[n][j]), "s"(inf2));
    SB();
#pragma unroll
    for (int j = 0; j < 8; ++j)
#pragma unroll
      for (int n = 0; n < 2; ++n) z[n][j] = __builtin_elementwise_fma(z[n][j], t[n][j], xx[n][j]);
    SB();
#pragma unroll
    for (int i = 0; i < 16; ++i)
      *(unsigned*)(ob + h * 512 + c * 4 + ((i & 3) + 8 * (i >> 2)) * 128) = pack_bf16(z[0][i >> 1][i & 1], z[1][i >> 1][i & 1]);
    SB();
    u32x4 v[4];
#pragma unroll
    for (int i = 0; i < 4; ++i) v[i] = *(const u32x4*)(ob + i * 1024 + lane * 16);
    SB();
#pragma unroll
    for (int i = 0; i < 4; ++i)
      __builtin_amdgcn_raw_buffer_store_b128(v[i], __builtin_amdgcn_make_buffer_rsrc(out + (size_t)node0 * 64, 0, nn * 128, 0x00020000),
                                             (unsigned)(lane * 16 + i * 1024), 0, 17);
  }
}

extern "C" void kernel_launch(void* const* d_in, const int* in_sizes, int n_in, void* d_out, int out_size, void* d_ws,
                              size_t ws_size, hipStream_t stream) {
  const uint16_t* x = (const uint16_t*)d_in[0];
  const char* eattr = (const char*)d_in[1];
  const int* src = (const int*)d_in[2];
  const int* dst = (const int*)d_in[3];
  const uint16_t* wa = (const uint16_t*)d_in[4];
  const uint16_t* wb = (const uint16_t*)d_in[5];
  const uint16_t* wc = (const uint16_t*)d_in[6];
  const uint16_t* wu = (const uint16_t*)d_in[7];
  const uint16_t* wv = (const uint16_t*)d_in[8];
  const int N = in_sizes[0] / 64;
  const int E = in_sizes[2];
  const unsigned magic = (unsigned)((0x100000000ull * NB * NG) / (unsigned)N) + 1u;
  const int CS = ((E + NQ - 1) / NQ + 3) & ~3;
  char* ws = (char*)d_ws;
  k1_partition<<<NB, K1T, 0, stream>>>(dst, src, ws, E, CS, (unsigned)N, magic, wa, wb, wc, wu, wv);
  k2_main<<<NB, K2T, 0, stream>>>(ws, x, eattr, (uint16_t*)d_out, N);
}
```
